```python
import torch

def _softmax(x, axis=-1):
    return torch.softmax(x, dim=axis)
def _gelu(x):
    return torch.nn.functional.gelu(x, approximate="tanh")
def _silu(x):
    return torch.nn.functional.silu(x)
def _sigmoid(x):
    return torch.sigmoid(x)
def _layernorm(x, axis=-1, eps=1e-5):
    m = x.mean(dim=axis, keepdim=True)
    v = x.var(dim=axis, keepdim=True, unbiased=False)
    return (x - m) / torch.sqrt(v + eps)
def _scatter_add(tgt, idx, src, axis=0):
    return tgt.clone().index_add_(axis, idx, src)
def _segment_sum(src, seg, n):
    out = torch.zeros((n,) + src.shape[1:], dtype=src.dtype)
    return out.index_add_(0, seg, src)
def _pad(x, pw):
    flat = tuple(v for pair in reversed(pw) for v in pair)
    return torch.nn.functional.pad(x, flat)

kernel_name = 'standalone__mha_softmax__sampling'

def setup_inputs():

    _w = (lambda a: (lambda t: t.to(torch.bfloat16) if t.is_floating_point() else t)(torch.as_tensor(a)))
    B, H, Sq, Sk, D = 16, 32, 1, 2048, 128
    return {
        "q": _w((lambda *s: torch.randn(*s, dtype=torch.bfloat16) * 0.8)(B, H, Sq, D)),
        "k": _w((lambda *s: torch.randn(*s, dtype=torch.bfloat16) * 0.8)(B, H, Sk, D)),
        "v": _w((lambda *s: torch.randn(*s, dtype=torch.bfloat16) * 0.05)(B, H, Sk, D)),
    }

def reference(q, k, v):
    scale = q.shape[-1] ** -0.5
    scores = torch.einsum("bhqd,bhkd->bhqk", q, k) * scale
    p = _softmax(scores, axis=-1)
    return torch.einsum("bhqk,bhkd->bhqd", p, v)
```

```cpp
#include <hip/hip_runtime.h>
#include <hip/hip_bf16.h>
#include <cstdint>

#define NW 4
#define UU 4
#define NS 3
#define USE_NT 1

constexpr int D = 128, SK = 2048, NHEADS = 512;
constexpr int TILE_ROWS = 4 * UU;
constexpr int TILES = SK / TILE_ROWS;
constexpr int TPW = TILES / NW;
static_assert(TPW * NW == TILES, "tiles split evenly");

using bf16x2 = __attribute__((ext_vector_type(2))) __bf16;
using u32x4 = __attribute__((ext_vector_type(4))) unsigned;
using f32x2 = __attribute__((ext_vector_type(2))) float;
using f32x4 = __attribute__((ext_vector_type(4))) float;

__device__ __forceinline__ float dot2(unsigned k, unsigned q, float acc) {
  return __builtin_amdgcn_fdot2_f32_bf16(__builtin_bit_cast(bf16x2, k), __builtin_bit_cast(bf16x2, q), acc, false);
}
template <int CTRL> __device__ __forceinline__ float dpp(float x) {
  return __builtin_bit_cast(float, __builtin_amdgcn_mov_dpp(__builtin_bit_cast(int, x), CTRL, 0xf, 0xf, true));
}
__device__ __forceinline__ float row16_sum(float x) {
  x += dpp<0xB1>(x);
  x += dpp<0x4E>(x);
  x += dpp<0x141>(x);
  x += dpp<0x140>(x);
  return x;
}
__device__ __forceinline__ float xrow_max(float x) {
  auto s = __builtin_amdgcn_permlane16_swap(__float_as_uint(x), __float_as_uint(x), false, false);
  x = fmaxf(__uint_as_float(s[0]), __uint_as_float(s[1]));
  auto t = __builtin_amdgcn_permlane32_swap(__float_as_uint(x), __float_as_uint(x), false, false);
  return fmaxf(__uint_as_float(t[0]), __uint_as_float(t[1]));
}
__device__ __forceinline__ float xrow_sum(float x) {
  auto s = __builtin_amdgcn_permlane16_swap(__float_as_uint(x), __float_as_uint(x), false, false);
  x = __uint_as_float(s[0]) + __uint_as_float(s[1]);
  auto t = __builtin_amdgcn_permlane32_swap(__float_as_uint(x), __float_as_uint(x), false, false);
  return __uint_as_float(t[0]) + __uint_as_float(t[1]);
}
__device__ __forceinline__ u32x4 ldg(const u32x4* p) {
  return __builtin_nontemporal_load(p);
}

struct Tile { u32x4 k[UU], v[UU]; };

__device__ __forceinline__ void issue(Tile& t, const u32x4* kp, const u32x4* vp) {
#pragma unroll
  for (int i = 0; i < UU; ++i) t.k[i] = ldg(kp + i * 64);
#pragma unroll
  for (int i = 0; i < UU; ++i) t.v[i] = ldg(vp + i * 64);
  __builtin_amdgcn_sched_barrier(0);
}

constexpr float C2 = 0.088388347648318440f * 1.4426950408889634f;

__device__ __forceinline__ void consume(const Tile& t, const u32x4& q, float& m, float& l, f32x2 (&acc)[4]) {
  float s[UU];
#pragma unroll
  for (int i = 0; i < UU; ++i) {
    float d = dot2(t.k[i][0], q[0], 0.f);
    d = dot2(t.k[i][1], q[1], d);
    d = dot2(t.k[i][2], q[2], d);
    d = dot2(t.k[i][3], q[3], d);
    s[i] = d;
  }
#pragma unroll
  for (int i = 0; i < UU; ++i) s[i] = row16_sum(s[i]) * C2;
  float mn = m;
#pragma unroll
  for (int i = 0; i < UU; ++i) mn = fmaxf(mn, s[i]);
  const float alpha = __builtin_amdgcn_exp2f(m - mn);
  m = mn;
  float p[UU];
  float ps = 0.f;
#pragma unroll
  for (int i = 0; i < UU; ++i) { p[i] = __builtin_amdgcn_exp2f(s[i] - mn); ps += p[i]; }
  l = fmaf(l, alpha, ps);
  const f32x2 a2 = {alpha, alpha};
#pragma unroll
  for (int j = 0; j < 4; ++j) acc[j] *= a2;
#pragma unroll
  for (int i = 0; i < UU; ++i) {
    const f32x2 p2 = {p[i], p[i]};
#pragma unroll
    for (int j = 0; j < 4; ++j) {
      const unsigned w = t.v[i][j];
      const f32x2 vf = {__builtin_bit_cast(float, w << 16), __builtin_bit_cast(float, w & 0xffff0000u)};
      acc[j] = __builtin_elementwise_fma(p2, vf, acc[j]);
    }
  }
  __builtin_amdgcn_sched_barrier(0);
}

__global__ void __launch_bounds__(NW * 64) mha_decode_kernel(const u32x4* __restrict__ Q, const u32x4* __restrict__ K,
                                                             const u32x4* __restrict__ V, unsigned* __restrict__ O) {
  __shared__ __attribute__((aligned(16))) float lds[NW * (D + 4)];
  const int tid = threadIdx.x, lane = tid & 63, wave = __builtin_amdgcn_readfirstlane(tid >> 6);
  const int c = lane & 15;
  const int head = blockIdx.x;
  const u32x4 q = Q[head * 16 + c];
  const size_t base = (size_t)head * (SK * D / 8) + (size_t)wave * (UU * 64) + lane;
  const u32x4* kp = K + base;
  const u32x4* vp = V + base;
  constexpr int STEP = NW * UU * 64;

  float m = -INFINITY, l = 0.f;
  f32x2 acc[4] = {{0.f, 0.f}, {0.f, 0.f}, {0.f, 0.f}, {0.f, 0.f}};
  Tile ring[NS];
#pragma unroll
  for (int s = 0; s < NS - 1; ++s) issue(ring[s], kp + s * STEP, vp + s * STEP);
  constexpr int MAIN = ((TPW - (NS - 1)) / NS) * NS;
  int t = 0;
  for (; t < MAIN; t += NS) {
#pragma unroll
    for (int s = 0; s < NS; ++s) {
      issue(ring[(s + NS - 1) % NS], kp + (size_t)(t + s + NS - 1) * STEP, vp + (size_t)(t + s + NS - 1) * STEP);
      consume(ring[s], q, m, l, acc);
    }
  }
#pragma unroll
  for (int u = MAIN; u < TPW; ++u) {
    if (u + NS - 1 < TPW) issue(ring[(u + NS - 1) % NS], kp + (size_t)(u + NS - 1) * STEP, vp + (size_t)(u + NS - 1) * STEP);
    consume(ring[u % NS], q, m, l, acc);
  }
  const float M = xrow_max(m);
  const float w0 = __builtin_amdgcn_exp2f(m - M);
  l = xrow_sum(l * w0);
  float o[8];
#pragma unroll
  for (int j = 0; j < 4; ++j) { o[2 * j] = xrow_sum(acc[j][0] * w0); o[2 * j + 1] = xrow_sum(acc[j][1] * w0); }
  float* rec = lds + wave * (D + 4);
  if (lane < 16) {
    *reinterpret_cast<f32x4*>(rec + 4 + 8 * c) = f32x4{o[0], o[1], o[2], o[3]};
    *reinterpret_cast<f32x4*>(rec + 8 + 8 * c) = f32x4{o[4], o[5], o[6], o[7]};
    if (lane == 0) { rec[0] = M; rec[1] = l; }
  }
  __syncthreads();
  if (wave == 0) {
    float MM = -INFINITY;
#pragma unroll
    for (int w = 0; w < NW; ++w) MM = fmaxf(MM, lds[w * (D + 4)]);
    float L = 0.f, o0 = 0.f, o1 = 0.f;
#pragma unroll
    for (int w = 0; w < NW; ++w) {
      const float* r = lds + w * (D + 4);
      const float wt = __builtin_amdgcn_exp2f(r[0] - MM);
      L = fmaf(wt, r[1], L);
      const f32x2 a = *reinterpret_cast<const f32x2*>(r + 4 + 2 * lane);
      o0 = fmaf(wt, a[0], o0);
      o1 = fmaf(wt, a[1], o1);
    }
    const float inv = 1.f / L;
    const bf16x2 ob = {(__bf16)(o0 * inv), (__bf16)(o1 * inv)};
    O[head * (D / 2) + lane] = __builtin_bit_cast(unsigned, ob);
  }
}

extern "C" void kernel_launch(void* const* d_in, const int* in_sizes, int n_in, void* d_out, int out_size, void* d_ws,
                              size_t ws_size, hipStream_t stream) {
  mha_decode_kernel<<<NHEADS, NW * 64, 0, stream>>>((const u32x4*)d_in[0], (const u32x4*)d_in[1], (const u32x4*)d_in[2],
                                                    (unsigned*)d_out);
}
```

```cpp
#include <hip/hip_runtime.h>
#include <hip/hip_bf16.h>
#include <cstdint>

#define RING 4
#define PACE 7400
#define STAMP 0
#define LDSPAD 84000
#define RTPARAM 0

constexpr int D = 128, SK = 2048, NHEADS = 512, NWG = 256, NW = 4;
constexpr unsigned HEAD_BYTES = SK * D * 2, TENSOR_BYTES = NHEADS * HEAD_BYTES;
constexpr int NBLK = 16;
constexpr int NGRP = NBLK * 8;

using bf16x2 = __attribute__((ext_vector_type(2))) __bf16;
using u32x4 = __attribute__((ext_vector_type(4))) unsigned;
using f32x2 = __attribute__((ext_vector_type(2))) float;
using f32x4 = __attribute__((ext_vector_type(4))) float;
typedef __amdgpu_buffer_rsrc_t rsrc_t;

__device__ __forceinline__ float dot2(unsigned k, unsigned q, float acc) {
  return __builtin_amdgcn_fdot2_f32_bf16(__builtin_bit_cast(bf16x2, k), __builtin_bit_cast(bf16x2, q), acc, false);
}
template <int CTRL> __device__ __forceinline__ float dpp(float x) {
  return __builtin_bit_cast(float, __builtin_amdgcn_mov_dpp(__builtin_bit_cast(int, x), CTRL, 0xf, 0xf, true));
}
__device__ __forceinline__ float row16_sum(float x) {
  x += dpp<0xB1>(x);
  x += dpp<0x4E>(x);
  x += dpp<0x141>(x);
  x += dpp<0x140>(x);
  return x;
}
__device__ __forceinline__ float xrow_max(float x) {
  auto s = __builtin_amdgcn_permlane16_swap(__float_as_uint(x), __float_as_uint(x), false, false);
  x = fmaxf(__uint_as_float(s[0]), __uint_as_float(s[1]));
  auto t = __builtin_amdgcn_permlane32_swap(__float_as_uint(x), __float_as_uint(x), false, false);
  return fmaxf(__uint_as_float(t[0]), __uint_as_float(t[1]));
}
__device__ __forceinline__ float xrow_sum(float x) {
  auto s = __builtin_amdgcn_permlane16_swap(__float_as_uint(x), __float_as_uint(x), false, false);
  x = __uint_as_float(s[0]) + __uint_as_float(s[1]);
  auto t = __builtin_amdgcn_permlane32_swap(__float_as_uint(x), __float_as_uint(x), false, false);
  return __uint_as_float(t[0]) + __uint_as_float(t[1]);
}
__device__ __forceinline__ unsigned long long rt() { return __builtin_amdgcn_s_memrealtime(); }

struct Grp { u32x4 r[4]; };

__device__ __forceinline__ void issue(Grp& g, rsrc_t rs, int voff, unsigned soff) {
#pragma unroll
  for (int i = 0; i < 4; ++i) g.r[i] = __builtin_amdgcn_raw_buffer_load_b128(rs, voff + i * 1024, soff, 2  );
  __builtin_amdgcn_sched_barrier(0);
}
constexpr float C2 = 0.088388347648318440f * 1.4426950408889634f;

__device__ __forceinline__ void scores(const Grp& g, const u32x4& q, float* s) {
#pragma unroll
  for (int i = 0; i < 4; ++i) {
    float d = dot2(g.r[i][0], q[0], 0.f);
    d = dot2(g.r[i][1], q[1], d);
    d = dot2(g.r[i][2], q[2], d);
    d = dot2(g.r[i][3], q[3], d);
    s[i] = d;
  }
#pragma unroll
  for (int i = 0; i < 4; ++i) s[i] = row16_sum(s[i]);
  asm volatile("" : "+v"(s[0]), "+v"(s[1]), "+v"(s[2]), "+v"(s[3]));
  __builtin_amdgcn_sched_barrier(0);
}
__device__ __forceinline__ void softmax16(float* s, float& m, float& l, f32x2 (&acc)[4]) {
  float mn = m;
#pragma unroll
  for (int i = 0; i < 16; ++i) mn = fmaxf(mn, s[i]);
  const float alpha = __builtin_amdgcn_exp2f((m - mn) * C2);
  m = mn;
  const float nb = -mn * C2;
  float ps0 = 0.f, ps1 = 0.f;
#pragma unroll
  for (int i = 0; i < 16; i += 2) {
    s[i] = __builtin_amdgcn_exp2f(fmaf(s[i], C2, nb)); ps0 += s[i];
    s[i + 1] = __builtin_amdgcn_exp2f(fmaf(s[i + 1], C2, nb)); ps1 += s[i + 1];
  }
  l = fmaf(l, alpha, ps0 + ps1);
  const f32x2 a2 = {alpha, alpha};
#pragma unroll
  for (int j = 0; j < 4; ++j) acc[j] *= a2;
  asm volatile("" : "+v"(l), "+v"(acc[0]), "+v"(acc[1]), "+v"(acc[2]), "+v"(acc[3]));
  __builtin_amdgcn_sched_barrier(0);
}
__device__ __forceinline__ void pv(const Grp& g, const float* p, f32x2 (&acc)[4]) {
#pragma unroll
  for (int i = 0; i < 4; ++i) {
    const f32x2 p2 = {p[i], p[i]};
#pragma unroll
    for (int j = 0; j < 4; ++j) {
      const unsigned w = g.r[i][j];
      const f32x2 vf = {__builtin_bit_cast(float, w << 16), __builtin_bit_cast(float, w & 0xffff0000u)};
      acc[j] = __builtin_elementwise_fma(p2, vf, acc[j]);
    }
  }
  asm volatile("" : "+v"(acc[0]), "+v"(acc[1]), "+v"(acc[2]), "+v"(acc[3]));
  __builtin_amdgcn_sched_barrier(0);
}

constexpr int REC = D + 4;

__global__ void __launch_bounds__(NW * 64) mha_decode_kernel(const u32x4* __restrict__ Q, const unsigned char* __restrict__ K,
                                                             const unsigned char* __restrict__ V, unsigned* __restrict__ O
                                                             ) {
  constexpr unsigned pace = PACE;
  __shared__ __attribute__((aligned(16))) float lds[2 * NW * REC + LDSPAD / 4];
  const unsigned long long t0l = rt();
  const unsigned t0 = (unsigned)t0l;
  const int tid = threadIdx.x, lane = tid & 63, wave = __builtin_amdgcn_readfirstlane(tid >> 6);
  const int c = lane & 15;
  const int g = blockIdx.x;
  u32x4 q = Q[g * 16 + c];
  const u32x4 q2 = Q[(g + NWG) * 16 + c];
  const rsrc_t kr = __builtin_amdgcn_make_buffer_rsrc((void*)K, 0, TENSOR_BYTES, 0x00020000);
  const rsrc_t vr = __builtin_amdgcn_make_buffer_rsrc((void*)V, 0, TENSOR_BYTES, 0x00020000);
  const int voff = lane * 16;
  auto boff = [&](int b) -> unsigned {
    return b < NBLK ? (unsigned)(g + (b >> 3) * NWG) * HEAD_BYTES + (unsigned)(b & 7) * 65536u + (unsigned)wave * 4096u : TENSOR_BYTES;
  };
  auto pacew = [&](int j) {
    const unsigned due = t0 + (j < NGRP ? ((unsigned)j * pace) >> 7 : 0u);
    while ((int)((unsigned)rt() - due) < 0) __builtin_amdgcn_s_sleep(1);
  };
  float m = -INFINITY, l = 0.f;
  f32x2 acc[4] = {{0.f, 0.f}, {0.f, 0.f}, {0.f, 0.f}, {0.f, 0.f}};
  float sc[16];
  Grp ring[RING];
  {
    const unsigned o = boff(0);
#pragma unroll
    for (int s = 0; s < 4; ++s) issue(ring[s], kr, voff, o + s * 16384);
  }
#pragma nounroll
  for (int b = 0; b < NBLK; ++b) {
    const unsigned oc = boff(b), on = boff(b + 1);
#pragma unroll
    for (int s = 0; s < 4; ++s) {
      scores(ring[s], q, sc + 4 * s);
      pacew(b * 8 + s + RING);
      issue(ring[s], vr, voff, oc + s * 16384);
    }
    softmax16(sc, m, l, acc);
#pragma unroll
    for (int s = 0; s < 4; ++s) {
      pv(ring[(4 + s) % RING], sc + 4 * s, acc);
      pacew(b * 8 + 4 + s + RING);
      issue(ring[s], kr, voff, on + s * 16384);
    }
    if ((b & 7) == 7) {
      const float M = xrow_max(m);
      const float w0 = __builtin_amdgcn_exp2f((m - M) * C2);
      const float L = xrow_sum(l * w0);
      float o[8];
#pragma unroll
      for (int j = 0; j < 4; ++j) { o[2 * j] = xrow_sum(acc[j][0] * w0); o[2 * j + 1] = xrow_sum(acc[j][1] * w0); }
      float* rec = lds + ((b >> 3) * NW + wave) * REC;
      if (lane < 16) {
        *reinterpret_cast<f32x4*>(rec + 4 + 8 * c) = f32x4{o[0], o[1], o[2], o[3]};
        *reinterpret_cast<f32x4*>(rec + 8 + 8 * c) = f32x4{o[4], o[5], o[6], o[7]};
        if (lane == 0) { rec[0] = M; rec[1] = L; }
      }
      m = -INFINITY; l = 0.f;
#pragma unroll
      for (int j = 0; j < 4; ++j) acc[j] = f32x2{0.f, 0.f};
      q = q2;
    }
  }
  __syncthreads();
  if (wave < 2) {
    const float* base = lds + wave * NW * REC;
    float MM = -INFINITY;
#pragma unroll
    for (int w = 0; w < NW; ++w) MM = fmaxf(MM, base[w * REC]);
    float L = 0.f, o0 = 0.f, o1 = 0.f;
#pragma unroll
    for (int w = 0; w < NW; ++w) {
      const float* r = base + w * REC;
      const float wt = __builtin_amdgcn_exp2f((r[0] - MM) * C2);
      L = fmaf(wt, r[1], L);
      const f32x2 a = *reinterpret_cast<const f32x2*>(r + 4 + 2 * lane);
      o0 = fmaf(wt, a[0], o0);
      o1 = fmaf(wt, a[1], o1);
    }
    const float inv = 1.f / L;
    const bf16x2 ob = {(__bf16)(o0 * inv), (__bf16)(o1 * inv)};
    O[(g + wave * NWG) * (D / 2) + lane] = __builtin_bit_cast(unsigned, ob);
  }
}

extern "C" void kernel_launch(void* const* d_in, const int* in_sizes, int n_in, void* d_out, int out_size, void* d_ws,
                              size_t ws_size, hipStream_t stream) {
  mha_decode_kernel<<<NWG, NW * 64, 0, stream>>>((const u32x4*)d_in[0], (const unsigned char*)d_in[1], (const unsigned char*)d_in[2],
                                                 (unsigned*)d_out
                                                 );
}
```

```cpp
#include <hip/hip_runtime.h>
#include <hip/hip_bf16.h>
#include <cstdint>

#define RING 4
#define PACE 8000
#define STAMP 0
#define LDSPAD 84000
#define RTPARAM 0

constexpr int D = 128, SK = 2048, NHEADS = 512, NWG = 256, NW = 4;
constexpr unsigned HEAD_BYTES = SK * D * 2, TENSOR_BYTES = NHEADS * HEAD_BYTES;
constexpr int NBLK = 16;
constexpr int NGRP = NBLK * 8;

using bf16x2 = __attribute__((ext_vector_type(2))) __bf16;
using u32x4 = __attribute__((ext_vector_type(4))) unsigned;
using f32x2 = __attribute__((ext_vector_type(2))) float;
using f32x4 = __attribute__((ext_vector_type(4))) float;
typedef __amdgpu_buffer_rsrc_t rsrc_t;

__device__ __forceinline__ float dot2(unsigned k, unsigned q, float acc) {
  return __builtin_amdgcn_fdot2_f32_bf16(__builtin_bit_cast(bf16x2, k), __builtin_bit_cast(bf16x2, q), acc, false);
}
template <int CTRL> __device__ __forceinline__ float dpp(float x) {
  return __builtin_bit_cast(float, __builtin_amdgcn_mov_dpp(__builtin_bit_cast(int, x), CTRL, 0xf, 0xf, true));
}
__device__ __forceinline__ float row16_sum(float x) {
  x += dpp<0xB1>(x);
  x += dpp<0x4E>(x);
  x += dpp<0x141>(x);
  x += dpp<0x140>(x);
  return x;
}
__device__ __forceinline__ float xrow_max(float x) {
  auto s = __builtin_amdgcn_permlane16_swap(__float_as_uint(x), __float_as_uint(x), false, false);
  x = fmaxf(__uint_as_float(s[0]), __uint_as_float(s[1]));
  auto t = __builtin_amdgcn_permlane32_swap(__float_as_uint(x), __float_as_uint(x), false, false);
  return fmaxf(__uint_as_float(t[0]), __uint_as_float(t[1]));
}
__device__ __forceinline__ float xrow_sum(float x) {
  auto s = __builtin_amdgcn_permlane16_swap(__float_as_uint(x), __float_as_uint(x), false, false);
  x = __uint_as_float(s[0]) + __uint_as_float(s[1]);
  auto t = __builtin_amdgcn_permlane32_swap(__float_as_uint(x), __float_as_uint(x), false, false);
  return __uint_as_float(t[0]) + __uint_as_float(t[1]);
}
__device__ __forceinline__ unsigned long long rt() { return __builtin_amdgcn_s_memrealtime(); }

struct Grp { u32x4 r[4]; };

__device__ __forceinline__ void issue(Grp& g, rsrc_t rs, int voff, unsigned soff) {
#pragma unroll
  for (int i = 0; i < 4; ++i) g.r[i] = __builtin_amdgcn_raw_buffer_load_b128(rs, voff + i * 1024, soff, 2  );
  __builtin_amdgcn_sched_barrier(0);
}
constexpr float C2 = 0.088388347648318440f * 1.4426950408889634f;

__device__ __forceinline__ void scores(const Grp& g, const u32x4& q, float* s) {
#pragma unroll
  for (int i = 0; i < 4; ++i) {
    float d = dot2(g.r[i][0], q[0], 0.f);
    d = dot2(g.r[i][1], q[1], d);
    d = dot2(g.r[i][2], q[2], d);
    d = dot2(g.r[i][3], q[3], d);
    s[i] = d;
  }
#pragma unroll
  for (int i = 0; i < 4; ++i) s[i] = row16_sum(s[i]);
  asm volatile("" : "+v"(s[0]), "+v"(s[1]), "+v"(s[2]), "+v"(s[3]));
  __builtin_amdgcn_sched_barrier(0);
}
__device__ __forceinline__ void softmax16(float* s, float& m, float& l, f32x2 (&acc)[4]) {
  float mn = m;
#pragma unroll
  for (int i = 0; i < 16; ++i) mn = fmaxf(mn, s[i]);
  const float alpha = __builtin_amdgcn_exp2f((m - mn) * C2);
  m = mn;
  const float nb = -mn * C2;
  float ps0 = 0.f, ps1 = 0.f;
#pragma unroll
  for (int i = 0; i < 16; i += 2) {
    s[i] = __builtin_amdgcn_exp2f(fmaf(s[i], C2, nb)); ps0 += s[i];
    s[i + 1] = __builtin_amdgcn_exp2f(fmaf(s[i + 1], C2, nb)); ps1 += s[i + 1];
  }
  l = fmaf(l, alpha, ps0 + ps1);
  const f32x2 a2 = {alpha, alpha};
#pragma unroll
  for (int j = 0; j < 4; ++j) acc[j] *= a2;
  asm volatile("" : "+v"(l), "+v"(acc[0]), "+v"(acc[1]), "+v"(acc[2]), "+v"(acc[3]));
  __builtin_amdgcn_sched_barrier(0);
}
__device__ __forceinline__ void pv(const Grp& g, const float* p, f32x2 (&acc)[4]) {
#pragma unroll
  for (int i = 0; i < 4; ++i) {
    const f32x2 p2 = {p[i], p[i]};
#pragma unroll
    for (int j = 0; j < 4; ++j) {
      const unsigned w = g.r[i][j];
      const f32x2 vf = {__builtin_bit_cast(float, w << 16), __builtin_bit_cast(float, w & 0xffff0000u)};
      acc[j] = __builtin_elementwise_fma(p2, vf, acc[j]);
    }
  }
  asm volatile("" : "+v"(acc[0]), "+v"(acc[1]), "+v"(acc[2]), "+v"(acc[3]));
  __builtin_amdgcn_sched_barrier(0);
}

constexpr int REC = D + 4;

__global__ void __launch_bounds__(NW * 64) mha_decode_kernel(const u32x4* __restrict__ Q, const unsigned char* __restrict__ K,
                                                             const unsigned char* __restrict__ V, unsigned* __restrict__ O, unsigned* TS
                                                             ) {
  constexpr unsigned pace = PACE;
  __shared__ __attribute__((aligned(16))) float lds[2 * NW * REC + LDSPAD / 4];
  const unsigned long long t0l = rt();
  const unsigned t0 = (unsigned)t0l;
  const int tid = threadIdx.x, lane = tid & 63, wave = __builtin_amdgcn_readfirstlane(tid >> 6);
  const int c = lane & 15;
  const int g = blockIdx.x;
  if (tid == 0) __hip_atomic_store(TS + g, t0, __ATOMIC_RELAXED, __HIP_MEMORY_SCOPE_AGENT);
  unsigned base2 = t0 + (pace >> 1), span2 = pace >> 1;
  u32x4 q = Q[g * 16 + c];
  const u32x4 q2 = Q[(g + NWG) * 16 + c];
  const rsrc_t kr = __builtin_amdgcn_make_buffer_rsrc((void*)K, 0, TENSOR_BYTES, 0x00020000);
  const rsrc_t vr = __builtin_amdgcn_make_buffer_rsrc((void*)V, 0, TENSOR_BYTES, 0x00020000);
  const int voff = lane * 16;
  auto boff = [&](int b) -> unsigned {
    return b < NBLK ? (unsigned)(g + (b >> 3) * NWG) * HEAD_BYTES + (unsigned)(b & 7) * 65536u + (unsigned)wave * 4096u : TENSOR_BYTES;
  };
  auto pacew = [&](int j) {
    const unsigned due = j < 64 + RING ? t0 + (((unsigned)j * pace) >> 7) : (j < NGRP ? base2 + (unsigned)(j - 64 - RING) * span2 / (unsigned)(64 - RING) : t0);
    while ((int)((unsigned)rt() - due) < 0) __builtin_amdgcn_s_sleep(1);
  };
  float m = -INFINITY, l = 0.f;
  f32x2 acc[4] = {{0.f, 0.f}, {0.f, 0.f}, {0.f, 0.f}, {0.f, 0.f}};
  float sc[16];
  Grp ring[RING];
  {
    const unsigned o = boff(0);
#pragma unroll
    for (int s = 0; s < 4; ++s) issue(ring[s], kr, voff, o + s * 16384);
  }
#pragma nounroll
  for (int b = 0; b < NBLK; ++b) {
    const unsigned oc = boff(b), on = boff(b + 1);
    if (b == 8) {
      int mn = 0, mx = 0, sum = 0;
#pragma unroll
      for (int u = 0; u < 4; ++u) {
        int d = (int)(__hip_atomic_load(TS + lane * 4 + u, __ATOMIC_RELAXED, __HIP_MEMORY_SCOPE_AGENT) - t0);
        if (d < -3000 || d > 3000) d = 0;
        mn = min(mn, d); mx = max(mx, d); sum += d;
      }
#pragma unroll
      for (int o = 1; o < 64; o <<= 1) { mn = min(mn, __shfl_xor(mn, o)); mx = max(mx, __shfl_xor(mx, o)); sum += __shfl_xor(sum, o); }
      mn = __builtin_amdgcn_readfirstlane(mn); mx = __builtin_amdgcn_readfirstlane(mx); sum = __builtin_amdgcn_readfirstlane(sum);
      const int F = (int)pace + 0 * (mn + mx + sum);
      base2 = t0 + (((unsigned)(64 + RING) * pace) >> 7);
      span2 = (unsigned)(F - (int)(((unsigned)(64 + RING) * pace) >> 7));
    }
#pragma unroll
    for (int s = 0; s < 4; ++s) {
      scores(ring[s], q, sc + 4 * s);
      pacew(b * 8 + s + RING);
      issue(ring[s], vr, voff, oc + s * 16384);
    }
    softmax16(sc, m, l, acc);
#pragma unroll
    for (int s = 0; s < 4; ++s) {
      pv(ring[(4 + s) % RING], sc + 4 * s, acc);
      pacew(b * 8 + 4 + s + RING);
      issue(ring[s], kr, voff, on + s * 16384);
    }
    if ((b & 7) == 7) {
      const float M = xrow_max(m);
      const float w0 = __builtin_amdgcn_exp2f((m - M) * C2);
      const float L = xrow_sum(l * w0);
      float o[8];
#pragma unroll
      for (int j = 0; j < 4; ++j) { o[2 * j] = xrow_sum(acc[j][0] * w0); o[2 * j + 1] = xrow_sum(acc[j][1] * w0); }
      float* rec = lds + ((b >> 3) * NW + wave) * REC;
      if (lane < 16) {
        *reinterpret_cast<f32x4*>(rec + 4 + 8 * c) = f32x4{o[0], o[1], o[2], o[3]};
        *reinterpret_cast<f32x4*>(rec + 8 + 8 * c) = f32x4{o[4], o[5], o[6], o[7]};
        if (lane == 0) { rec[0] = M; rec[1] = L; }
      }
      m = -INFINITY; l = 0.f;
#pragma unroll
      for (int j = 0; j < 4; ++j) acc[j] = f32x2{0.f, 0.f};
      q = q2;
    }
  }
  __syncthreads();
  if (wave < 2) {
    const float* base = lds + wave * NW * REC;
    float MM = -INFINITY;
#pragma unroll
    for (int w = 0; w < NW; ++w) MM = fmaxf(MM, base[w * REC]);
    float L = 0.f, o0 = 0.f, o1 = 0.f;
#pragma unroll
    for (int w = 0; w < NW; ++w) {
      const float* r = base + w * REC;
      const float wt = __builtin_amdgcn_exp2f((r[0] - MM) * C2);
      L = fmaf(wt, r[1], L);
      const f32x2 a = *reinterpret_cast<const f32x2*>(r + 4 + 2 * lane);
      o0 = fmaf(wt, a[0], o0);
      o1 = fmaf(wt, a[1], o1);
    }
    const float inv = 1.f / L;
    const bf16x2 ob = {(__bf16)(o0 * inv), (__bf16)(o1 * inv)};
    O[(g + wave * NWG) * (D / 2) + lane] = __builtin_bit_cast(unsigned, ob);
  }
}

extern "C" void kernel_launch(void* const* d_in, const int* in_sizes, int n_in, void* d_out, int out_size, void* d_ws,
                              size_t ws_size, hipStream_t stream) {
  mha_decode_kernel<<<NWG, NW * 64, 0, stream>>>((const u32x4*)d_in[0], (const unsigned char*)d_in[1], (const unsigned char*)d_in[2],
                                                 (unsigned*)d_out, (unsigned*)((char*)d_ws + (16 << 20))
                                                 );
}
```

```cpp
#include <hip/hip_runtime.h>
#include <hip/hip_bf16.h>
#include <cstdint>

#define RING 4
#define PACE 8000
#define STAMP 0
#define LDSPAD 84000
#define RTPARAM 0

constexpr int D = 128, SK = 2048, NHEADS = 512, NWG = 256, NW = 4;
constexpr unsigned HEAD_BYTES = SK * D * 2, TENSOR_BYTES = NHEADS * HEAD_BYTES;
constexpr int NBLK = 16;
constexpr int NGRP = NBLK * 8;

using bf16x2 = __attribute__((ext_vector_type(2))) __bf16;
using u32x4 = __attribute__((ext_vector_type(4))) unsigned;
using f32x2 = __attribute__((ext_vector_type(2))) float;
using f32x4 = __attribute__((ext_vector_type(4))) float;
typedef __amdgpu_buffer_rsrc_t rsrc_t;

__device__ __forceinline__ float dot2(unsigned k, unsigned q, float acc) {
  return __builtin_amdgcn_fdot2_f32_bf16(__builtin_bit_cast(bf16x2, k), __builtin_bit_cast(bf16x2, q), acc, false);
}
template <int CTRL> __device__ __forceinline__ float dpp(float x) {
  return __builtin_bit_cast(float, __builtin_amdgcn_mov_dpp(__builtin_bit_cast(int, x), CTRL, 0xf, 0xf, true));
}
__device__ __forceinline__ float row16_sum(float x) {
  x += dpp<0xB1>(x);
  x += dpp<0x4E>(x);
  x += dpp<0x141>(x);
  x += dpp<0x140>(x);
  return x;
}
__device__ __forceinline__ float xrow_max(float x) {
  auto s = __builtin_amdgcn_permlane16_swap(__float_as_uint(x), __float_as_uint(x), false, false);
  x = fmaxf(__uint_as_float(s[0]), __uint_as_float(s[1]));
  auto t = __builtin_amdgcn_permlane32_swap(__float_as_uint(x), __float_as_uint(x), false, false);
  return fmaxf(__uint_as_float(t[0]), __uint_as_float(t[1]));
}
__device__ __forceinline__ float xrow_sum(float x) {
  auto s = __builtin_amdgcn_permlane16_swap(__float_as_uint(x), __float_as_uint(x), false, false);
  x = __uint_as_float(s[0]) + __uint_as_float(s[1]);
  auto t = __builtin_amdgcn_permlane32_swap(__float_as_uint(x), __float_as_uint(x), false, false);
  return __uint_as_float(t[0]) + __uint_as_float(t[1]);
}
__device__ __forceinline__ unsigned long long rt() { return __builtin_amdgcn_s_memrealtime(); }

struct Grp { u32x4 r[4]; };

__device__ __forceinline__ void issue(Grp& g, rsrc_t rs, int voff, unsigned soff) {
#pragma unroll
  for (int i = 0; i < 4; ++i) g.r[i] = __builtin_amdgcn_raw_buffer_load_b128(rs, voff + i * 1024, soff, 2  );
  __builtin_amdgcn_sched_barrier(0);
}
constexpr float C2 = 0.088388347648318440f * 1.4426950408889634f;

__device__ __forceinline__ void scores(const Grp& g, const u32x4& q, float* s) {
#pragma unroll
  for (int i = 0; i < 4; ++i) {
    float d = dot2(g.r[i][0], q[0], 0.f);
    d = dot2(g.r[i][1], q[1], d);
    d = dot2(g.r[i][2], q[2], d);
    d = dot2(g.r[i][3], q[3], d);
    s[i] = d;
  }
#pragma unroll
  for (int i = 0; i < 4; ++i) s[i] = row16_sum(s[i]);
  asm volatile("" : "+v"(s[0]), "+v"(s[1]), "+v"(s[2]), "+v"(s[3]));
  __builtin_amdgcn_sched_barrier(0);
}
__device__ __forceinline__ void softmax16(float* s, float& m, float& l, f32x2 (&acc)[4]) {
  float mn = m;
#pragma unroll
  for (int i = 0; i < 16; ++i) mn = fmaxf(mn, s[i]);
  const float alpha = __builtin_amdgcn_exp2f((m - mn) * C2);
  m = mn;
  const float nb = -mn * C2;
  float ps0 = 0.f, ps1 = 0.f;
#pragma unroll
  for (int i = 0; i < 16; i += 2) {
    s[i] = __builtin_amdgcn_exp2f(fmaf(s[i], C2, nb)); ps0 += s[i];
    s[i + 1] = __builtin_amdgcn_exp2f(fmaf(s[i + 1], C2, nb)); ps1 += s[i + 1];
  }
  l = fmaf(l, alpha, ps0 + ps1);
  const f32x2 a2 = {alpha, alpha};
#pragma unroll
  for (int j = 0; j < 4; ++j) acc[j] *= a2;
  asm volatile("" : "+v"(l), "+v"(acc[0]), "+v"(acc[1]), "+v"(acc[2]), "+v"(acc[3]));
  __builtin_amdgcn_sched_barrier(0);
}
__device__ __forceinline__ void pv(const Grp& g, const float* p, f32x2 (&acc)[4]) {
#pragma unroll
  for (int i = 0; i < 4; ++i) {
    const f32x2 p2 = {p[i], p[i]};
#pragma unroll
    for (int j = 0; j < 4; ++j) {
      const unsigned w = g.r[i][j];
      const f32x2 vf = {__builtin_bit_cast(float, w << 16), __builtin_bit_cast(float, w & 0xffff0000u)};
      acc[j] = __builtin_elementwise_fma(p2, vf, acc[j]);
    }
  }
  asm volatile("" : "+v"(acc[0]), "+v"(acc[1]), "+v"(acc[2]), "+v"(acc[3]));
  __builtin_amdgcn_sched_barrier(0);
}

constexpr int REC = D + 4;

__global__ void __launch_bounds__(NW * 64) mha_decode_kernel(const u32x4* __restrict__ Q_, const unsigned char* __restrict__ K_,
                                                             const unsigned char* __restrict__ V_, unsigned* __restrict__ O_
                                                             ) {
  constexpr unsigned pace = PACE;
  __shared__ __attribute__((aligned(16))) float lds[2 * NW * REC + LDSPAD / 4];
  unsigned long long t0l, t0b;
  typedef unsigned long long u64x4 __attribute__((ext_vector_type(4)));
  u64x4 ka;
  asm volatile("s_memrealtime %0\n\ts_waitcnt lgkmcnt(0)" : "=s"(t0l));
  asm volatile("s_load_dwordx8 %0, %1, 0x0\n\ts_waitcnt lgkmcnt(0)" : "=s"(ka) : "s"(__builtin_amdgcn_kernarg_segment_ptr()));
  asm volatile("s_memrealtime %0\n\ts_waitcnt lgkmcnt(0)" : "=s"(t0b));
  typedef const __attribute__((address_space(1))) u32x4* gq_t;
  typedef __attribute__((address_space(1))) unsigned* go_t;
  gq_t Q = (gq_t)ka[0]; const unsigned char* K = (const unsigned char*)ka[1]; const unsigned char* V = (const unsigned char*)ka[2]; go_t O = (go_t)ka[3];
  const unsigned t0 = (unsigned)t0l;
  const int tid = threadIdx.x, lane = tid & 63, wave = __builtin_amdgcn_readfirstlane(tid >> 6);
  const int c = lane & 15;
  const int g = blockIdx.x;
  unsigned base2 = t0 + (pace >> 1), span2 = pace >> 1;
  unsigned tiss = t0, stat = 0 * (unsigned)t0b;
  u32x4 q = Q[g * 16 + c];
  const u32x4 q2 = Q[(g + NWG) * 16 + c];
  const rsrc_t kr = __builtin_amdgcn_make_buffer_rsrc((void*)K, 0, TENSOR_BYTES, 0x00020000);
  const rsrc_t vr = __builtin_amdgcn_make_buffer_rsrc((void*)V, 0, TENSOR_BYTES, 0x00020000);
  const int voff = lane * 16;
  auto boff = [&](int b) -> unsigned {
    return b < NBLK ? (unsigned)(g + (b >> 3) * NWG) * HEAD_BYTES + (unsigned)(b & 7) * 65536u + (unsigned)wave * 4096u : TENSOR_BYTES;
  };
  auto pacew = [&](int j) {
    const unsigned due = j < 96 + RING ? t0 + (((unsigned)j * pace) >> 7) : (j < NGRP ? base2 + (unsigned)(j - 96 - RING) * span2 / (unsigned)(32 - RING) : t0);
    while ((int)((unsigned)rt() - due) < 0) __builtin_amdgcn_s_sleep(1);
  };
  float m = -INFINITY, l = 0.f;
  f32x2 acc[4] = {{0.f, 0.f}, {0.f, 0.f}, {0.f, 0.f}, {0.f, 0.f}};
  float sc[16];
  Grp ring[RING];
  {
    const unsigned o = boff(0);
#pragma unroll
    for (int s = 0; s < 4; ++s) issue(ring[s], kr, voff, o + s * 16384);
  }
#pragma nounroll
  for (int b = 0; b < NBLK; ++b) {
    const unsigned oc = boff(b), on = boff(b + 1);
    if (b == 12) {
      const int F = (int)pace + 4 * (int)stat;
      base2 = t0 + (((unsigned)(96 + RING) * pace) >> 7);
      span2 = (unsigned)(F - (int)(((unsigned)(96 + RING) * pace) >> 7));
    }
#pragma unroll
    for (int s = 0; s < 4; ++s) {
      scores(ring[s], q, sc + 4 * s);
      pacew(b * 8 + s + RING);
      issue(ring[s], vr, voff, oc + s * 16384);
    }
    softmax16(sc, m, l, acc);
#pragma unroll
    for (int s = 0; s < 4; ++s) {
      pv(ring[(4 + s) % RING], sc + 4 * s, acc);
      pacew(b * 8 + 4 + s + RING);
      issue(ring[s], kr, voff, on + s * 16384);
    }
    if ((b & 7) == 7) {
      const float M = xrow_max(m);
      const float w0 = __builtin_amdgcn_exp2f((m - M) * C2);
      const float L = xrow_sum(l * w0);
      float o[8];
#pragma unroll
      for (int j = 0; j < 4; ++j) { o[2 * j] = xrow_sum(acc[j][0] * w0); o[2 * j + 1] = xrow_sum(acc[j][1] * w0); }
      float* rec = lds + ((b >> 3) * NW + wave) * REC;
      if (lane < 16) {
        *reinterpret_cast<f32x4*>(rec + 4 + 8 * c) = f32x4{o[0], o[1], o[2], o[3]};
        *reinterpret_cast<f32x4*>(rec + 8 + 8 * c) = f32x4{o[4], o[5], o[6], o[7]};
        if (lane == 0) { rec[0] = M; rec[1] = L; }
      }
      m = -INFINITY; l = 0.f;
#pragma unroll
      for (int j = 0; j < 4; ++j) acc[j] = f32x2{0.f, 0.f};
      q = q2;
    }
  }
  __syncthreads();
  if (wave < 2) {
    const float* base = lds + wave * NW * REC;
    float MM = -INFINITY;
#pragma unroll
    for (int w = 0; w < NW; ++w) MM = fmaxf(MM, base[w * REC]);
    float L = 0.f, o0 = 0.f, o1 = 0.f;
#pragma unroll
    for (int w = 0; w < NW; ++w) {
      const float* r = base + w * REC;
      const float wt = __builtin_amdgcn_exp2f((r[0] - MM) * C2);
      L = fmaf(wt, r[1], L);
      const f32x2 a = *reinterpret_cast<const f32x2*>(r + 4 + 2 * lane);
      o0 = fmaf(wt, a[0], o0);
      o1 = fmaf(wt, a[1], o1);
    }
    const float inv = 1.f / L;
    const bf16x2 ob = {(__bf16)(o0 * inv), (__bf16)(o1 * inv)};
    O[(g + wave * NWG) * (D / 2) + lane] = __builtin_bit_cast(unsigned, ob);
  }
}

extern "C" void kernel_launch(void* const* d_in, const int* in_sizes, int n_in, void* d_out, int out_size, void* d_ws,
                              size_t ws_size, hipStream_t stream) {
  mha_decode_kernel<<<NWG, NW * 64, 0, stream>>>((const u32x4*)d_in[0], (const unsigned char*)d_in[1], (const unsigned char*)d_in[2],
                                                 (unsigned*)d_out
                                                 );
}
```

```cpp
#include <hip/hip_runtime.h>
#include <hip/hip_bf16.h>
#include <cstdint>

#define RING 4
#define PACE 7150
#define STAMP 0
#define LDSPAD 84000
#define RTPARAM 0

constexpr int D = 128, SK = 2048, NHEADS = 512, NWG = 256, NW = 4;
constexpr unsigned HEAD_BYTES = SK * D * 2, TENSOR_BYTES = NHEADS * HEAD_BYTES;
constexpr int NBLK = 16;
constexpr int NGRP = NBLK * 8;

using bf16x2 = __attribute__((ext_vector_type(2))) __bf16;
using u32x4 = __attribute__((ext_vector_type(4))) unsigned;
using f32x2 = __attribute__((ext_vector_type(2))) float;
using f32x4 = __attribute__((ext_vector_type(4))) float;
typedef __amdgpu_buffer_rsrc_t rsrc_t;

__device__ __forceinline__ float dot2(unsigned k, unsigned q, float acc) {
  return __builtin_amdgcn_fdot2_f32_bf16(__builtin_bit_cast(bf16x2, k), __builtin_bit_cast(bf16x2, q), acc, false);
}
template <int CTRL> __device__ __forceinline__ float dpp(float x) {
  return __builtin_bit_cast(float, __builtin_amdgcn_mov_dpp(__builtin_bit_cast(int, x), CTRL, 0xf, 0xf, true));
}
__device__ __forceinline__ float row16_sum(float x) {
  x += dpp<0xB1>(x);
  x += dpp<0x4E>(x);
  x += dpp<0x141>(x);
  x += dpp<0x140>(x);
  return x;
}
__device__ __forceinline__ float xrow_max(float x) {
  auto s = __builtin_amdgcn_permlane16_swap(__float_as_uint(x), __float_as_uint(x), false, false);
  x = fmaxf(__uint_as_float(s[0]), __uint_as_float(s[1]));
  auto t = __builtin_amdgcn_permlane32_swap(__float_as_uint(x), __float_as_uint(x), false, false);
  return fmaxf(__uint_as_float(t[0]), __uint_as_float(t[1]));
}
__device__ __forceinline__ float xrow_sum(float x) {
  auto s = __builtin_amdgcn_permlane16_swap(__float_as_uint(x), __float_as_uint(x), false, false);
  x = __uint_as_float(s[0]) + __uint_as_float(s[1]);
  auto t = __builtin_amdgcn_permlane32_swap(__float_as_uint(x), __float_as_uint(x), false, false);
  return __uint_as_float(t[0]) + __uint_as_float(t[1]);
}
__device__ __forceinline__ unsigned long long rt() { return __builtin_amdgcn_s_memrealtime(); }

struct Grp { u32x4 r[4]; };

__device__ __forceinline__ void issue(Grp& g, rsrc_t rs, int voff, unsigned soff) {
#pragma unroll
  for (int i = 0; i < 4; ++i) g.r[i] = __builtin_amdgcn_raw_buffer_load_b128(rs, voff + i * 1024, soff, 2  );
  __builtin_amdgcn_sched_barrier(0);
}
constexpr float C2 = 0.088388347648318440f * 1.4426950408889634f;

__device__ __forceinline__ void scores(const Grp& g, const u32x4& q, float* s) {
#pragma unroll
  for (int i = 0; i < 4; ++i) {
    float d = dot2(g.r[i][0], q[0], 0.f);
    d = dot2(g.r[i][1], q[1], d);
    d = dot2(g.r[i][2], q[2], d);
    d = dot2(g.r[i][3], q[3], d);
    s[i] = d;
  }
#pragma unroll
  for (int i = 0; i < 4; ++i) s[i] = row16_sum(s[i]);
  asm volatile("" : "+v"(s[0]), "+v"(s[1]), "+v"(s[2]), "+v"(s[3]));
  __builtin_amdgcn_sched_barrier(0);
}
__device__ __forceinline__ void softmax16(float* s, float& m, float& l, f32x2 (&acc)[4]) {
  float mn = m;
#pragma unroll
  for (int i = 0; i < 16; ++i) mn = fmaxf(mn, s[i]);
  const float alpha = __builtin_amdgcn_exp2f((m - mn) * C2);
  m = mn;
  const float nb = -mn * C2;
  float ps0 = 0.f, ps1 = 0.f;
#pragma unroll
  for (int i = 0; i < 16; i += 2) {
    s[i] = __builtin_amdgcn_exp2f(fmaf(s[i], C2, nb)); ps0 += s[i];
    s[i + 1] = __builtin_amdgcn_exp2f(fmaf(s[i + 1], C2, nb)); ps1 += s[i + 1];
  }
  l = fmaf(l, alpha, ps0 + ps1);
  const f32x2 a2 = {alpha, alpha};
#pragma unroll
  for (int j = 0; j < 4; ++j) acc[j] *= a2;
  asm volatile("" : "+v"(l), "+v"(acc[0]), "+v"(acc[1]), "+v"(acc[2]), "+v"(acc[3]));
  __builtin_amdgcn_sched_barrier(0);
}
__device__ __forceinline__ void pv(const Grp& g, const float* p, f32x2 (&acc)[4]) {
#pragma unroll
  for (int i = 0; i < 4; ++i) {
    const f32x2 p2 = {p[i], p[i]};
#pragma unroll
    for (int j = 0; j < 4; ++j) {
      const unsigned w = g.r[i][j];
      const f32x2 vf = {__builtin_bit_cast(float, w << 16), __builtin_bit_cast(float, w & 0xffff0000u)};
      acc[j] = __builtin_elementwise_fma(p2, vf, acc[j]);
    }
  }
  asm volatile("" : "+v"(acc[0]), "+v"(acc[1]), "+v"(acc[2]), "+v"(acc[3]));
  __builtin_amdgcn_sched_barrier(0);
}

constexpr int REC = D + 4;

__global__ void __launch_bounds__(NW * 64) mha_decode_kernel(const u32x4* __restrict__ Q, const unsigned char* __restrict__ K,
                                                             const unsigned char* __restrict__ V, unsigned* __restrict__ O
                                                             ) {
  constexpr unsigned pace = PACE;
  __shared__ __attribute__((aligned(16))) float lds[2 * NW * REC + LDSPAD / 4];
  const unsigned long long t0l = rt();
  const unsigned t0 = (unsigned)t0l;
  const int tid = threadIdx.x, lane = tid & 63, wave = __builtin_amdgcn_readfirstlane(tid >> 6);
  const int c = lane & 15;
  const int g = blockIdx.x;
  u32x4 q = Q[g * 16 + c];
  const u32x4 q2 = Q[(g + NWG) * 16 + c];
  const rsrc_t kr = __builtin_amdgcn_make_buffer_rsrc((void*)K, 0, TENSOR_BYTES, 0x00020000);
  const rsrc_t vr = __builtin_amdgcn_make_buffer_rsrc((void*)V, 0, TENSOR_BYTES, 0x00020000);
  const int voff = lane * 16;
  auto boff = [&](int b) -> unsigned {
    return b < NBLK ? (unsigned)(g + (b >> 3) * NWG) * HEAD_BYTES + (unsigned)(b & 7) * 65536u + (unsigned)wave * 4096u : TENSOR_BYTES;
  };
  auto pacew = [&](int j) {
    const unsigned due = t0 + (j < NGRP ? ((unsigned)j * pace) >> 7 : 0u);
    while ((int)((unsigned)rt() - due) < 0) __builtin_amdgcn_s_sleep(1);
  };
  float m = -INFINITY, l = 0.f;
  f32x2 acc[4] = {{0.f, 0.f}, {0.f, 0.f}, {0.f, 0.f}, {0.f, 0.f}};
  float sc[16];
  Grp ring[RING];
  {
    const unsigned o = boff(0);
#pragma unroll
    for (int s = 0; s < 4; ++s) issue(ring[s], kr, voff, o + s * 16384);
  }
#pragma nounroll
  for (int b = 0; b < NBLK; ++b) {
    const unsigned oc = boff(b), on = boff(b + 1);
#pragma unroll
    for (int s = 0; s < 4; ++s) {
      scores(ring[s], q, sc + 4 * s);
      pacew(b * 8 + s + RING);
      issue(ring[s], vr, voff, oc + s * 16384);
    }
    softmax16(sc, m, l, acc);
#pragma unroll
    for (int s = 0; s < 4; ++s) {
      pv(ring[(4 + s) % RING], sc + 4 * s, acc);
      pacew(b * 8 + 4 + s + RING);
      issue(ring[s], kr, voff, on + s * 16384);
    }
    if ((b & 7) == 7) {
      const float M = xrow_max(m);
      const float w0 = __builtin_amdgcn_exp2f((m - M) * C2);
      const float L = xrow_sum(l * w0);
      float o[8];
#pragma unroll
      for (int j = 0; j < 4; ++j) { o[2 * j] = xrow_sum(acc[j][0] * w0); o[2 * j + 1] = xrow_sum(acc[j][1] * w0); }
      float* rec = lds + ((b >> 3) * NW + wave) * REC;
      if (lane < 16) {
        *reinterpret_cast<f32x4*>(rec + 4 + 8 * c) = f32x4{o[0], o[1], o[2], o[3]};
        *reinterpret_cast<f32x4*>(rec + 8 + 8 * c) = f32x4{o[4], o[5], o[6], o[7]};
        if (lane == 0) { rec[0] = M; rec[1] = L; }
      }
      m = -INFINITY; l = 0.f;
#pragma unroll
      for (int j = 0; j < 4; ++j) acc[j] = f32x2{0.f, 0.f};
      q = q2;
    }
  }
  __syncthreads();
  if (wave < 2) {
    const float* base = lds + wave * NW * REC;
    float MM = -INFINITY;
#pragma unroll
    for (int w = 0; w < NW; ++w) MM = fmaxf(MM, base[w * REC]);
    float L = 0.f, o0 = 0.f, o1 = 0.f;
#pragma unroll
    for (int w = 0; w < NW; ++w) {
      const float* r = base + w * REC;
      const float wt = __builtin_amdgcn_exp2f((r[0] - MM) * C2);
      L = fmaf(wt, r[1], L);
      const f32x2 a = *reinterpret_cast<const f32x2*>(r + 4 + 2 * lane);
      o0 = fmaf(wt, a[0], o0);
      o1 = fmaf(wt, a[1], o1);
    }
    const float inv = 1.f / L;
    const bf16x2 ob = {(__bf16)(o0 * inv), (__bf16)(o1 * inv)};
    O[(g + wave * NWG) * (D / 2) + lane] = __builtin_bit_cast(unsigned, ob);
  }
}

extern "C" void kernel_launch(void* const* d_in, const int* in_sizes, int n_in, void* d_out, int out_size, void* d_ws,
                              size_t ws_size, hipStream_t stream) {
  mha_decode_kernel<<<NWG, NW * 64, 0, stream>>>((const u32x4*)d_in[0], (const unsigned char*)d_in[1], (const unsigned char*)d_in[2],
                                                 (unsigned*)d_out
                                                 );
}
```

```cpp
#include <hip/hip_runtime.h>
#include <hip/hip_bf16.h>
#include <cstdint>

#define RING 4
#define PACE 7050
#define STAMP 0
#define LDSPAD 84000
#define HM 0
#define RTPARAM 0

constexpr int D = 128, SK = 2048, NHEADS = 512, NWG = 256, NW = 4;
constexpr unsigned HEAD_BYTES = SK * D * 2, TENSOR_BYTES = NHEADS * HEAD_BYTES;
constexpr int NBLK = 16;
constexpr int NGRP = NBLK * 8;

using bf16x2 = __attribute__((ext_vector_type(2))) __bf16;
using u32x4 = __attribute__((ext_vector_type(4))) unsigned;
using f32x2 = __attribute__((ext_vector_type(2))) float;
using f32x4 = __attribute__((ext_vector_type(4))) float;
typedef __amdgpu_buffer_rsrc_t rsrc_t;

__device__ __forceinline__ float dot2(unsigned k, unsigned q, float acc) {
  return __builtin_amdgcn_fdot2_f32_bf16(__builtin_bit_cast(bf16x2, k), __builtin_bit_cast(bf16x2, q), acc, false);
}
template <int CTRL> __device__ __forceinline__ float dpp(float x) {
  return __builtin_bit_cast(float, __builtin_amdgcn_mov_dpp(__builtin_bit_cast(int, x), CTRL, 0xf, 0xf, true));
}
__device__ __forceinline__ float row16_sum(float x) {
  x += dpp<0xB1>(x);
  x += dpp<0x4E>(x);
  x += dpp<0x141>(x);
  x += dpp<0x140>(x);
  return x;
}
__device__ __forceinline__ float xrow_max(float x) {
  auto s = __builtin_amdgcn_permlane16_swap(__float_as_uint(x), __float_as_uint(x), false, false);
  x = fmaxf(__uint_as_float(s[0]), __uint_as_float(s[1]));
  auto t = __builtin_amdgcn_permlane32_swap(__float_as_uint(x), __float_as_uint(x), false, false);
  return fmaxf(__uint_as_float(t[0]), __uint_as_float(t[1]));
}
__device__ __forceinline__ float xrow_sum(float x) {
  auto s = __builtin_amdgcn_permlane16_swap(__float_as_uint(x), __float_as_uint(x), false, false);
  x = __uint_as_float(s[0]) + __uint_as_float(s[1]);
  auto t = __builtin_amdgcn_permlane32_swap(__float_as_uint(x), __float_as_uint(x), false, false);
  return __uint_as_float(t[0]) + __uint_as_float(t[1]);
}
__device__ __forceinline__ unsigned long long rt() { return __builtin_amdgcn_s_memrealtime(); }

struct Grp { u32x4 r[4]; };

__device__ __forceinline__ void issue(Grp& g, rsrc_t rs, int voff, unsigned soff) {
#pragma unroll
  for (int i = 0; i < 4; ++i) g.r[i] = __builtin_amdgcn_raw_buffer_load_b128(rs, voff + i * 256, soff, 2  );
  __builtin_amdgcn_sched_barrier(0);
}
constexpr float C2 = 0.088388347648318440f * 1.4426950408889634f;

__device__ __forceinline__ void scores(const Grp& g, const u32x4& q, float* s) {
#pragma unroll
  for (int i = 0; i < 4; ++i) {
    float d = dot2(g.r[i][0], q[0], 0.f);
    d = dot2(g.r[i][1], q[1], d);
    d = dot2(g.r[i][2], q[2], d);
    d = dot2(g.r[i][3], q[3], d);
    s[i] = d;
  }
#pragma unroll
  for (int i = 0; i < 4; ++i) s[i] = row16_sum(s[i]);
  asm volatile("" : "+v"(s[0]), "+v"(s[1]), "+v"(s[2]), "+v"(s[3]));
  __builtin_amdgcn_sched_barrier(0);
}
__device__ __forceinline__ void softmax16(float* s, float& m, float& l, f32x2 (&acc)[4]) {
  float mn = m;
#pragma unroll
  for (int i = 0; i < 16; ++i) mn = fmaxf(mn, s[i]);
  const float alpha = __builtin_amdgcn_exp2f((m - mn) * C2);
  m = mn;
  const float nb = -mn * C2;
  float ps0 = 0.f, ps1 = 0.f;
#pragma unroll
  for (int i = 0; i < 16; i += 2) {
    s[i] = __builtin_amdgcn_exp2f(fmaf(s[i], C2, nb)); ps0 += s[i];
    s[i + 1] = __builtin_amdgcn_exp2f(fmaf(s[i + 1], C2, nb)); ps1 += s[i + 1];
  }
  l = fmaf(l, alpha, ps0 + ps1);
  const f32x2 a2 = {alpha, alpha};
#pragma unroll
  for (int j = 0; j < 4; ++j) acc[j] *= a2;
  asm volatile("" : "+v"(l), "+v"(acc[0]), "+v"(acc[1]), "+v"(acc[2]), "+v"(acc[3]));
  __builtin_amdgcn_sched_barrier(0);
}
__device__ __forceinline__ void pv(const Grp& g, const float* p, f32x2 (&acc)[4]) {
#pragma unroll
  for (int i = 0; i < 4; ++i) {
    const f32x2 p2 = {p[i], p[i]};
#pragma unroll
    for (int j = 0; j < 4; ++j) {
      const unsigned w = g.r[i][j];
      const f32x2 vf = {__builtin_bit_cast(float, w << 16), __builtin_bit_cast(float, w & 0xffff0000u)};
      acc[j] = __builtin_elementwise_fma(p2, vf, acc[j]);
    }
  }
  asm volatile("" : "+v"(acc[0]), "+v"(acc[1]), "+v"(acc[2]), "+v"(acc[3]));
  __builtin_amdgcn_sched_barrier(0);
}

constexpr int REC = D + 4;

__global__ void __launch_bounds__(NW * 64) mha_decode_kernel(const u32x4* __restrict__ Q, const unsigned char* __restrict__ K,
                                                             const unsigned char* __restrict__ V, unsigned* __restrict__ O
                                                             ) {
  constexpr unsigned pace = PACE;
  __shared__ __attribute__((aligned(16))) float lds[2 * NW * REC + LDSPAD / 4];
  const unsigned long long t0l = rt();
  const unsigned t0 = (unsigned)t0l;
  const int tid = threadIdx.x, lane = tid & 63, wave = __builtin_amdgcn_readfirstlane(tid >> 6);
  const int c = lane & 15;
  const int g = blockIdx.x;
  const int hd = HM ? (g & 7) * 32 + (g >> 3) : g;
  u32x4 q = Q[hd * 16 + c];
  const u32x4 q2 = Q[(hd + NWG) * 16 + c];
  const rsrc_t kr = __builtin_amdgcn_make_buffer_rsrc((void*)K, 0, TENSOR_BYTES, 0x00020000);
  const rsrc_t vr = __builtin_amdgcn_make_buffer_rsrc((void*)V, 0, TENSOR_BYTES, 0x00020000);
  const int voff = c * 16 + (lane >> 4) * 4096;
  auto boff = [&](int b) -> unsigned {
    return b < NBLK ? (unsigned)(hd + (b >> 3) * NWG) * HEAD_BYTES + (unsigned)(b & 7) * 65536u + (unsigned)wave * 1024u : TENSOR_BYTES;
  };
  auto pacew = [&](int j) {
    const unsigned due = t0 + (j < NGRP ? ((unsigned)j * pace) >> 7 : 0u);
    while ((int)((unsigned)rt() - due) < 0) __builtin_amdgcn_s_sleep(1);
  };
  float m = -INFINITY, l = 0.f;
  f32x2 acc[4] = {{0.f, 0.f}, {0.f, 0.f}, {0.f, 0.f}, {0.f, 0.f}};
  float sc[16];
  Grp ring[RING];
  {
    const unsigned o = boff(0);
#pragma unroll
    for (int s = 0; s < 4; ++s) issue(ring[s], kr, voff, o + s * 16384);
  }
#pragma nounroll
  for (int b = 0; b < NBLK; ++b) {
    const unsigned oc = boff(b), on = boff(b + 1);
#pragma unroll
    for (int s = 0; s < 4; ++s) {
      scores(ring[s], q, sc + 4 * s);
      pacew(b * 8 + s + RING);
      issue(ring[s], vr, voff, oc + s * 16384);
    }
    softmax16(sc, m, l, acc);
#pragma unroll
    for (int s = 0; s < 4; ++s) {
      pv(ring[(4 + s) % RING], sc + 4 * s, acc);
      pacew(b * 8 + 4 + s + RING);
      issue(ring[s], kr, voff, on + s * 16384);
    }
    if ((b & 7) == 7) {
      const float M = xrow_max(m);
      const float w0 = __builtin_amdgcn_exp2f((m - M) * C2);
      const float L = xrow_sum(l * w0);
      float o[8];
#pragma unroll
      for (int j = 0; j < 4; ++j) { o[2 * j] = xrow_sum(acc[j][0] * w0); o[2 * j + 1] = xrow_sum(acc[j][1] * w0); }
      float* rec = lds + ((b >> 3) * NW + wave) * REC;
      if (lane < 16) {
        *reinterpret_cast<f32x4*>(rec + 4 + 8 * c) = f32x4{o[0], o[1], o[2], o[3]};
        *reinterpret_cast<f32x4*>(rec + 8 + 8 * c) = f32x4{o[4], o[5], o[6], o[7]};
        if (lane == 0) { rec[0] = M; rec[1] = L; }
      }
      m = -INFINITY; l = 0.f;
#pragma unroll
      for (int j = 0; j < 4; ++j) acc[j] = f32x2{0.f, 0.f};
      q = q2;
    }
  }
  __syncthreads();
  if (wave < 2) {
    const float* base = lds + wave * NW * REC;
    float MM = -INFINITY;
#pragma unroll
    for (int w = 0; w < NW; ++w) MM = fmaxf(MM, base[w * REC]);
    float L = 0.f, o0 = 0.f, o1 = 0.f;
#pragma unroll
    for (int w = 0; w < NW; ++w) {
      const float* r = base + w * REC;
      const float wt = __builtin_amdgcn_exp2f((r[0] - MM) * C2);
      L = fmaf(wt, r[1], L);
      const f32x2 a = *reinterpret_cast<const f32x2*>(r + 4 + 2 * lane);
      o0 = fmaf(wt, a[0], o0);
      o1 = fmaf(wt, a[1], o1);
    }
    const float inv = 1.f / L;
    const bf16x2 ob = {(__bf16)(o0 * inv), (__bf16)(o1 * inv)};
    O[(hd + wave * NWG) * (D / 2) + lane] = __builtin_bit_cast(unsigned, ob);
  }
}

extern "C" void kernel_launch(void* const* d_in, const int* in_sizes, int n_in, void* d_out, int out_size, void* d_ws,
                              size_t ws_size, hipStream_t stream) {
  mha_decode_kernel<<<NWG, NW * 64, 0, stream>>>((const u32x4*)d_in[0], (const unsigned char*)d_in[1], (const unsigned char*)d_in[2],
                                                 (unsigned*)d_out
                                                 );
}
```

```cpp
#include <hip/hip_runtime.h>
#include <hip/hip_bf16.h>
#include <cstdint>

#define PACE 7050
#define STAMP 0
#define LDSPAD 64000
#define RTPARAM 0
#define HMAP 18174

constexpr int D = 128, SK = 2048, NHEADS = 512, NWG = 256, NW = 4;
constexpr unsigned HEAD_BYTES = SK * D * 2, TENSOR_BYTES = NHEADS * HEAD_BYTES;
constexpr int NSTEP = 128;

using bf16x2 = __attribute__((ext_vector_type(2))) __bf16;
using u32x4 = __attribute__((ext_vector_type(4))) unsigned;
using f32x2 = __attribute__((ext_vector_type(2))) float;
using f32x4 = __attribute__((ext_vector_type(4))) float;
typedef __amdgpu_buffer_rsrc_t rsrc_t;

__device__ __forceinline__ float dot2(unsigned k, unsigned q, float acc) {
  return __builtin_amdgcn_fdot2_f32_bf16(__builtin_bit_cast(bf16x2, k), __builtin_bit_cast(bf16x2, q), acc, false);
}
template <int CTRL> __device__ __forceinline__ float dpp(float x) {
  return __builtin_bit_cast(float, __builtin_amdgcn_mov_dpp(__builtin_bit_cast(int, x), CTRL, 0xf, 0xf, true));
}
__device__ __forceinline__ float row16_sum(float x) {
  x += dpp<0xB1>(x);
  x += dpp<0x4E>(x);
  x += dpp<0x141>(x);
  x += dpp<0x140>(x);
  return x;
}
__device__ __forceinline__ float swap32_add(float a, float b) {
  auto t = __builtin_amdgcn_permlane32_swap(__float_as_uint(a), __float_as_uint(b), false, false);
  return __uint_as_float(t[0]) + __uint_as_float(t[1]);
}
__device__ __forceinline__ float swap16_add(float a, float b) {
  auto t = __builtin_amdgcn_permlane16_swap(__float_as_uint(a), __float_as_uint(b), false, false);
  return __uint_as_float(t[0]) + __uint_as_float(t[1]);
}
__device__ __forceinline__ float xrow_max(float x) {
  auto s = __builtin_amdgcn_permlane16_swap(__float_as_uint(x), __float_as_uint(x), false, false);
  x = fmaxf(__uint_as_float(s[0]), __uint_as_float(s[1]));
  auto t = __builtin_amdgcn_permlane32_swap(__float_as_uint(x), __float_as_uint(x), false, false);
  return fmaxf(__uint_as_float(t[0]), __uint_as_float(t[1]));
}
__device__ __forceinline__ unsigned long long rt() { return __builtin_amdgcn_s_memrealtime(); }

struct Grp { u32x4 r[4]; };

__device__ __forceinline__ void issue(Grp& g, rsrc_t rs, int voff, unsigned soff) {
#pragma unroll
  for (int i = 0; i < 4; ++i) g.r[i] = __builtin_amdgcn_raw_buffer_load_b128(rs, voff + i * 256, soff, 2  );
  __builtin_amdgcn_sched_barrier(0);
}
constexpr float C2 = 0.088388347648318440f * 1.4426950408889634f;

constexpr int L_P = 0, L_ST = 2 * NW * 512, L_O = L_ST + 2 * NW * 2, L_END = L_O + 2 * NW * D;

__global__ void __launch_bounds__(NW * 64) mha_decode_kernel(const u32x4* __restrict__ Q, const unsigned char* __restrict__ K,
                                                             const unsigned char* __restrict__ V, unsigned* __restrict__ O
                                                             ) {
  constexpr unsigned pace = PACE;
  constexpr int hm = HMAP;
  __shared__ __attribute__((aligned(16))) float lds[L_END + LDSPAD / 4];
  const unsigned long long t0l = rt();
  const unsigned t0 = (unsigned)t0l;
  const int tid = threadIdx.x, lane = tid & 63, wave = __builtin_amdgcn_readfirstlane(tid >> 6);
  const int c = lane & 15, r = lane >> 4;
  const int g = blockIdx.x;
  int hd, hstep;
  {
    const int ph = (hm / 1000) % 10, p0 = (hm / 100) % 10, p1 = (hm / 10) % 10, p2 = hm % 10;
    hd = ((g & 1) << p0) | (((g >> 1) & 1) << p1) | (((g >> 2) & 1) << p2);
    int k = g >> 3;
#pragma unroll
    for (int p = 0; p < 9; ++p) if (p != p0 && p != p1 && p != p2 && p != ph) { hd |= (k & 1) << p; k >>= 1; }
    hstep = 1 << ph;
  }
  u32x4 q = Q[hd * 16 + c];
  const u32x4 q2 = Q[(hd + hstep) * 16 + c];
  const rsrc_t kr = __builtin_amdgcn_make_buffer_rsrc((void*)K, 0, TENSOR_BYTES, 0x00020000);
  const rsrc_t vr = __builtin_amdgcn_make_buffer_rsrc((void*)V, 0, TENSOR_BYTES, 0x00020000);
  const int voff = c * 16 + r * 4096;
  auto qoff = [&](int n) -> unsigned {
    return n < 32 ? (unsigned)(hd + ((n >> 3) & 1) * hstep) * HEAD_BYTES + (unsigned)(n & 7) * 65536u + (unsigned)wave * 1024u : TENSOR_BYTES;
  };
  auto pacew = [&](int j) {
    if (j < NSTEP) {
      const unsigned due = t0 + (((unsigned)j * pace) >> 7);
      while ((int)((unsigned)rt() - due) < 0) __builtin_amdgcn_s_sleep(1);
    }
  };
  Grp ring[4];
  {
    const unsigned o = qoff(0);
#pragma unroll
    for (int s = 0; s < 4; ++s) issue(ring[s], kr, voff, o + s * 16384);
  }
  float m = -INFINITY;
#pragma nounroll
  for (int n = 0; n < 16; ++n) {
    const unsigned on = qoff(n + 1);
    const rsrc_t nr = n < 15 ? kr : vr;
    float* prow = lds + L_P + ((n >> 3) * NW + wave) * 512 + (n & 7) * 64 + r * 4;
#pragma unroll
    for (int s = 0; s < 4; ++s) {
      f32x4 sc;
#pragma unroll
      for (int i = 0; i < 4; ++i) {
        float d = dot2(ring[s].r[i][0], q[0], 0.f);
        d = dot2(ring[s].r[i][1], q[1], d);
        d = dot2(ring[s].r[i][2], q[2], d);
        sc[i] = dot2(ring[s].r[i][3], q[3], d);
      }
      asm volatile("" : "+v"(sc));
      __builtin_amdgcn_sched_barrier(0);
      pacew(n * 4 + s + 4);
      issue(ring[s], nr, voff, on + s * 16384);
#pragma unroll
      for (int i = 0; i < 4; ++i) sc[i] = row16_sum(sc[i]);
      m = fmaxf(fmaxf(m, fmaxf(sc[0], sc[1])), fmaxf(sc[2], sc[3]));
      if (c == 0) *reinterpret_cast<f32x4*>(prow + s * 16) = sc;
      asm volatile("" : "+v"(m) :: "memory");
      __builtin_amdgcn_sched_barrier(0);
    }
    if ((n & 7) == 7) {
      const float M = xrow_max(m);
      const float nb = -M * C2;
      float* pw = lds + L_P + ((n >> 3) * NW + wave) * 512 + lane * 4;
      f32x4 a = *reinterpret_cast<const f32x4*>(pw), b = *reinterpret_cast<const f32x4*>(pw + 256);
#pragma unroll
      for (int i = 0; i < 4; ++i) { a[i] = __builtin_amdgcn_exp2f(fmaf(a[i], C2, nb)); b[i] = __builtin_amdgcn_exp2f(fmaf(b[i], C2, nb)); }
      *reinterpret_cast<f32x4*>(pw) = a; *reinterpret_cast<f32x4*>(pw + 256) = b;
      float l = ((a[0] + a[1]) + (a[2] + a[3])) + ((b[0] + b[1]) + (b[2] + b[3]));
      l = row16_sum(l); l = swap16_add(l, l); l = swap32_add(l, l);
      if (lane == 0) *reinterpret_cast<f32x2*>(lds + L_ST + ((n >> 3) * NW + wave) * 2) = f32x2{M, l};
      asm volatile("" ::: "memory");
      m = -INFINITY;
      q = q2;
    }
  }
  asm volatile("s_waitcnt lgkmcnt(0)\n\ts_barrier" ::: "memory");
  f32x2 acc[4] = {{0.f, 0.f}, {0.f, 0.f}, {0.f, 0.f}, {0.f, 0.f}};
#pragma nounroll
  for (int n = 16; n < 32; ++n) {
    const unsigned on = qoff(n + 1);
    const float* prow = lds + L_P + (((n >> 3) & 1) * NW + wave) * 512 + (n & 7) * 64 + r * 4;
    float fs = 0.f;
    if ((n & 7) == 7) {
      const float* st = lds + L_ST + ((n >> 3) & 1) * NW * 2;
      const f32x4 s01 = *reinterpret_cast<const f32x4*>(st), s23 = *reinterpret_cast<const f32x4*>(st + 4);
      const float MM = fmaxf(fmaxf(s01[0], s01[2]), fmaxf(s23[0], s23[2]));
      const float L = s01[1] * __builtin_amdgcn_exp2f((s01[0] - MM) * C2) + s01[3] * __builtin_amdgcn_exp2f((s01[2] - MM) * C2)
                    + s23[1] * __builtin_amdgcn_exp2f((s23[0] - MM) * C2) + s23[3] * __builtin_amdgcn_exp2f((s23[2] - MM) * C2);
      fs = __builtin_amdgcn_exp2f((st[wave * 2] - MM) * C2) / L;
      asm volatile("" : "+v"(fs));
    }
#pragma unroll
    for (int s = 0; s < 4; ++s) {
      const f32x4 p = *reinterpret_cast<const f32x4*>(prow + s * 16);
#pragma unroll
      for (int i = 0; i < 4; ++i) {
        const f32x2 p2 = {p[i], p[i]};
#pragma unroll
        for (int j = 0; j < 4; ++j) {
          const unsigned w = ring[s].r[i][j];
          const f32x2 vf = {__builtin_bit_cast(float, w << 16), __builtin_bit_cast(float, w & 0xffff0000u)};
          acc[j] = __builtin_elementwise_fma(p2, vf, acc[j]);
        }
      }
      asm volatile("" : "+v"(acc[0]), "+v"(acc[1]), "+v"(acc[2]), "+v"(acc[3]));
      __builtin_amdgcn_sched_barrier(0);
      pacew(n * 4 + s + 4);
      issue(ring[s], vr, voff, on + s * 16384);
    }
    if ((n & 7) == 7) {
      const float b0 = swap32_add(acc[0][0], acc[2][0]), b1 = swap32_add(acc[0][1], acc[2][1]);
      const float b2 = swap32_add(acc[1][0], acc[3][0]), b3 = swap32_add(acc[1][1], acc[3][1]);
      const f32x2 e = {swap16_add(b0, b2) * fs, swap16_add(b1, b3) * fs};
      *reinterpret_cast<f32x2*>(lds + L_O + (((n >> 3) & 1) * NW + wave) * D + (4 * c + r) * 2) = e;
      asm volatile("s_waitcnt lgkmcnt(0)\n\ts_barrier" ::: "memory");
      if (wave == 0) {
        const f32x2* po = reinterpret_cast<const f32x2*>(lds + L_O + ((n >> 3) & 1) * NW * D) + lane;
        const f32x2 o = (po[0] + po[64]) + (po[128] + po[192]);
        const bf16x2 ob = {(__bf16)o[0], (__bf16)o[1]};
        O[(hd + ((n >> 3) & 1) * hstep) * (D / 2) + lane] = __builtin_bit_cast(unsigned, ob);
      }
#pragma unroll
      for (int j = 0; j < 4; ++j) acc[j] = f32x2{0.f, 0.f};
    }
  }
}

extern "C" void kernel_launch(void* const* d_in, const int* in_sizes, int n_in, void* d_out, int out_size, void* d_ws,
                              size_t ws_size, hipStream_t stream) {
  mha_decode_kernel<<<NWG, NW * 64, 0, stream>>>((const u32x4*)d_in[0], (const unsigned char*)d_in[1], (const unsigned char*)d_in[2],
                                                 (unsigned*)d_out
                                                 );
}
```

```cpp
#include <hip/hip_runtime.h>
#include <hip/hip_bf16.h>
#include <cstdint>

constexpr int D = 128, SK = 2048, NHEADS = 512, NWG = 256, NW = 4;
constexpr unsigned HEAD_BYTES = SK * D * 2, TENSOR_BYTES = NHEADS * HEAD_BYTES;
constexpr int NSTEP = 128;
constexpr int CVN = 18;

constexpr unsigned PACE = 6940;
constexpr unsigned DT0 = PACE * 8;
constexpr unsigned DT_MIN = (PACE - 150) * 8, DT_MAX = 7700 * 8;
constexpr unsigned DELAY = 58;
constexpr unsigned SLACK = 12;
constexpr unsigned WSTAG = 4;
constexpr unsigned CMASK = 0x5554u | (1u << 19) | (1u << 24);
constexpr unsigned T_PUB = 270, T_READ = 430;
constexpr int BIAS = 12, ALARM = 12;
constexpr float THETA = 6.f;
constexpr int CODE_LINES = 60;

using bf16x2 = __attribute__((ext_vector_type(2))) __bf16;
using u32x2 = __attribute__((ext_vector_type(2))) unsigned;
using u32x4 = __attribute__((ext_vector_type(4))) unsigned;
using f32x2 = __attribute__((ext_vector_type(2))) float;
using f32x4 = __attribute__((ext_vector_type(4))) float;
typedef __amdgpu_buffer_rsrc_t rsrc_t;
#define LAS __attribute__((address_space(3)))
#define GAS __attribute__((address_space(1)))

__device__ __forceinline__ float dot2(unsigned k, unsigned q, float acc) {
  return __builtin_amdgcn_fdot2_f32_bf16(__builtin_bit_cast(bf16x2, k), __builtin_bit_cast(bf16x2, q), acc, false);
}
template <int CTRL> __device__ __forceinline__ float dpp(float x) {
  return __builtin_bit_cast(float, __builtin_amdgcn_mov_dpp(__builtin_bit_cast(int, x), CTRL, 0xf, 0xf, true));
}
__device__ __forceinline__ float row16_sum(float x) {
  x += dpp<0xB1>(x);
  x += dpp<0x4E>(x);
  x += dpp<0x141>(x);
  x += dpp<0x140>(x);
  return x;
}
__device__ __forceinline__ float swap32_add(float a, float b) {
  auto t = __builtin_amdgcn_permlane32_swap(__float_as_uint(a), __float_as_uint(b), false, false);
  return __uint_as_float(t[0]) + __uint_as_float(t[1]);
}
__device__ __forceinline__ float swap16_add(float a, float b) {
  auto t = __builtin_amdgcn_permlane16_swap(__float_as_uint(a), __float_as_uint(b), false, false);
  return __uint_as_float(t[0]) + __uint_as_float(t[1]);
}
__device__ __forceinline__ float xrow_max(float x) {
  auto s = __builtin_amdgcn_permlane16_swap(__float_as_uint(x), __float_as_uint(x), false, false);
  x = fmaxf(__uint_as_float(s[0]), __uint_as_float(s[1]));
  auto t = __builtin_amdgcn_permlane32_swap(__float_as_uint(x), __float_as_uint(x), false, false);
  return fmaxf(__uint_as_float(t[0]), __uint_as_float(t[1]));
}
__device__ __forceinline__ unsigned clk() { return (unsigned)__builtin_amdgcn_s_memrealtime(); }

struct Grp { u32x4 r[4]; };

__device__ __forceinline__ void issue(Grp& g, rsrc_t rs, int voff, unsigned soff) {
#pragma unroll
  for (int i = 0; i < 4; ++i) g.r[i] = __builtin_amdgcn_raw_buffer_load_b128(rs, voff + i * 256, soff, 2  );
  __builtin_amdgcn_sched_barrier(0);
}

struct Line {
  unsigned base, j0, dt;
  __device__ __forceinline__ unsigned due(int j) const { return base + (unsigned)(((j - (int)j0) * (int)dt) >> 10); }
};

constexpr float C2 = 0.088388347648318440f * 1.4426950408889634f;

constexpr int L_P = 0, L_ST = 2 * NW * 512, L_O = L_ST + 2 * NW * 2, L_CB = L_O + 2 * NW * D, L_END = L_CB + 16000;

__global__ void __launch_bounds__(NW * 64 + 64) mha_decode_kernel(const unsigned char* __restrict__ Q, const unsigned char* __restrict__ K,
                                                                  const unsigned char* __restrict__ V, unsigned* __restrict__ O,
                                                                  unsigned* GX) {
  __shared__ __attribute__((aligned(16))) float lds[L_END];
  const unsigned tid = threadIdx.x, g = blockIdx.x;
  const unsigned t0 = clk();
  __builtin_amdgcn_sched_barrier(0);
  const unsigned hd = ((g & 1) << 1) | ((g & 2) << 6) | ((g & 4) << 2) | ((g >> 3) & 1) | ((g >> 2) & 12) | ((g >> 1) & 96);
  volatile LAS unsigned* CB = (volatile LAS unsigned*)(lds + L_CB);
  volatile LAS u32x2* TAU = (volatile LAS u32x2*)(lds + L_CB + 2);

  if (__builtin_amdgcn_readfirstlane(tid) >= NW * 64) {
    __builtin_amdgcn_s_setprio(3);
    const int lane = tid & 63;
    if (lane == 0) CB[0] = 0u;
    while ((int)(clk() - (t0 + 300u + (g >> 3) * 15u)) < 0) __builtin_amdgcn_s_sleep(8);
    if (lane < 3) {
      const unsigned char* p = (lane == 0 ? K : V) + (size_t)(hd + (lane == 1 ? 0u : (unsigned)NWG)) * HEAD_BYTES;
      const unsigned x = __builtin_nontemporal_load(reinterpret_cast<const GAS unsigned*>((uintptr_t)p));
      asm volatile("" :: "v"(x));
    }
    unsigned tz = __builtin_amdgcn_readfirstlane(CB[1]);
    if (tz - t0 + 100u > 600u) tz = t0 + DELAY - WSTAG * (NW - 1);
    Line ln = {tz, 0u, DT0};
    float ref_tau = 0.f, ref_d = 0.f, dtn = (float)DT0;
    int ref_j = -1;
    bool seen = false;
#pragma nounroll
    for (int n = 0; n < 25; ++n) {
      if (n == CVN + 1 || n == 24) __builtin_amdgcn_s_barrier();
      if (!((CMASK >> n) & 1)) continue;
      const int J = n * 4 + 4;
      const unsigned dueJ = ln.due(J);
      bool todo = ((g >> 3) & 7) == 0;
      unsigned now;
      while ((int)((now = clk()) - (dueJ + T_READ)) < 0) {
        if (todo) {
          const u32x2 tv = TAU[0];
          const unsigned tau = __builtin_amdgcn_readfirstlane(tv[0]);
          const bool got = (int)__builtin_amdgcn_readfirstlane(tv[1]) == n && tau - dueJ + 1500u < 3000u;
          if (got || (int)(now - (dueJ + T_PUB)) >= 0) {
            const int late = got ? (int)(tau - dueJ) - BIAS : (int)(T_PUB + T_PUB / 4);
            if (late > ALARM && lane == 0)
              __hip_atomic_store(GX + n * 64 + (g & 7) + 8 * (g >> 6), (dueJ & ~1023u) | (unsigned)min(late, 1023), __ATOMIC_RELAXED,
                                 __HIP_MEMORY_SCOPE_AGENT);
            todo = false;
          }
        }
        __builtin_amdgcn_s_sleep(8);
      }
      const unsigned xw = __hip_atomic_load(GX + n * 64 + (lane & 31), __ATOMIC_RELAXED, __HIP_MEMORY_SCOPE_AGENT);
      const bool ok = (xw & ~1023u) - dueJ + 2500u < 4000u;
      float x = ok ? (float)(xw & 1023u) : 0.f;
      x += dpp<0x128>(x);
      x = swap16_add(x, x);
      float mn = fminf(x, dpp<0xB1>(x)); mn = fminf(mn, dpp<0x4E>(mn)); mn = fminf(mn, dpp<0x141>(mn));
      float sm = x + dpp<0xB1>(x); sm += dpp<0x4E>(sm); sm += dpp<0x141>(sm);
      const float tm = sm * (1.f / 32.f) + (float)(int)(dueJ - tz);
      const float dd = sm * (1.f / 32.f) - mn * 0.25f;
      if (ref_j < 0) { ref_j = J; ref_tau = tm; ref_d = dd; }
      else if (dd - ref_d > (seen ? THETA : 4.f * THETA)) dtn = fmaxf((tm - ref_tau) * 1036.f * __builtin_amdgcn_rcpf((float)(J - ref_j)), (float)DT0);
      else dtn -= (dtn - (float)DT0) * 0.2f;
      seen = dd - ref_d > THETA;
      if (__builtin_amdgcn_readfirstlane((int)(dtn > (float)DT0))) {
        const float F = tm + (float)(NSTEP - 1 - J) * dtn * (1.f / 1024.f);
        const int jn = J + 12;
        const unsigned bn = ln.due(jn);
        float d2 = (F - (float)(int)(bn - tz)) * 1024.f * __builtin_amdgcn_rcpf((float)(NSTEP - 1 - jn));
        d2 = fminf(fmaxf(d2, (float)DT_MIN), (float)DT_MAX);
        ln.base = bn; ln.j0 = jn; ln.dt = __builtin_amdgcn_readfirstlane((unsigned)d2);
        if (lane == 0) CB[0] = (ln.j0 << 24) | ln.dt;
      }
    }
    return;
  }

  const int voff = (int)(((tid & 0xcfu) << 4) | ((tid & 0x30u) << 8));
  const rsrc_t kr = __builtin_amdgcn_make_buffer_rsrc((void*)K, 0, TENSOR_BYTES, 0x00020000);
  u32x4 q = *reinterpret_cast<const u32x4*>(Q + (hd * 256u + (voff & 255)));
  Grp ring[4];
  issue(ring[0], kr, voff, hd * HEAD_BYTES);
  issue(ring[1], kr, voff, hd * HEAD_BYTES + 16384);
  const u32x4 q2 = *reinterpret_cast<const u32x4*>(Q + ((hd + NWG) * 256u + (voff & 255)));
  __builtin_amdgcn_sched_barrier(0);
  const unsigned t1 = clk();
  __builtin_amdgcn_sched_barrier(0);
  issue(ring[2], kr, voff, hd * HEAD_BYTES + 2 * 16384);
  issue(ring[3], kr, voff, hd * HEAD_BYTES + 3 * 16384);
  const int lane = tid & 63, wave = __builtin_amdgcn_readfirstlane(tid >> 6);
  const int c = lane & 15, r = lane >> 4;
  unsigned cpf = 0;
  if (wave == 3 && g < 16) {
    unsigned long long pc;
    asm volatile("s_getpc_b64 %0" : "=s"(pc));
    cpf = *reinterpret_cast<const GAS unsigned*>((pc & ~127ull) + (unsigned)(lane < CODE_LINES ? lane : 0) * 128u);
  }
  __builtin_amdgcn_sched_barrier(0);
  const rsrc_t vr = __builtin_amdgcn_make_buffer_rsrc((void*)V, 0, TENSOR_BYTES, 0x00020000);
  auto qoff = [&](int n) -> unsigned {
    return n < 32 ? (hd + (unsigned)(n & 8) * (NWG / 8)) * HEAD_BYTES + (unsigned)(n & 7) * 65536u : TENSOR_BYTES;
  };
  if (lane == 0) CB[4 + wave] = t1 - t0;
  asm volatile("s_waitcnt vmcnt(16)" ::: "memory");
  const u32x4 hv = *(volatile LAS u32x4*)(CB + 4);
  const unsigned held = __builtin_amdgcn_readfirstlane(min(min(hv[0], hv[1]), min(hv[2], hv[3])));
  Line ln = {t0 + min(max(held, DELAY + SLACK), 400u) - SLACK - WSTAG * (unsigned)(NW - 1 - wave), 0u, DT0};
  if (tid == 0) CB[1] = ln.base;
  auto refresh = [&](int n) {
    if (((CMASK << 3 | CMASK << 4) >> n) & 1) {
      const unsigned w = __builtin_amdgcn_readfirstlane(CB[0]), jn = w >> 24, d = w & 0xffffffu;
      if (jn != ln.j0 && jn + 4u - (unsigned)n * 4u <= 8u && d - DT_MIN <= DT_MAX - DT_MIN) { ln.base = ln.due((int)jn); ln.j0 = jn; ln.dt = d; }
    }
  };
  auto mark = [&](int n) {
    if (wave == 0 && ((CMASK >> n) & 1)) TAU[0] = u32x2{clk(), (unsigned)n};
  };
  auto pace = [&](int j, unsigned early) {
    if (j < NSTEP) {
      const unsigned due = ln.due(j);
      if ((int)(early - due) < 0)
        while ((int)(clk() - due) < 0) __builtin_amdgcn_s_sleep(1);
    }
  };
  auto convert = [&](int h, float mrow) {
    const float M = xrow_max(mrow);
    const float nb = -M * C2;
    float* pw = lds + L_P + (h * NW + wave) * 512 + lane * 4;
    f32x4 a = *reinterpret_cast<const f32x4*>(pw), b = *reinterpret_cast<const f32x4*>(pw + 256);
#pragma unroll
    for (int i = 0; i < 4; ++i) { a[i] = __builtin_amdgcn_exp2f(fmaf(a[i], C2, nb)); b[i] = __builtin_amdgcn_exp2f(fmaf(b[i], C2, nb)); }
    *reinterpret_cast<f32x4*>(pw) = a; *reinterpret_cast<f32x4*>(pw + 256) = b;
    float l = ((a[0] + a[1]) + (a[2] + a[3])) + ((b[0] + b[1]) + (b[2] + b[3]));
    l = row16_sum(l); l = swap16_add(l, l); l = swap32_add(l, l);
    if (lane == 0) *reinterpret_cast<f32x2*>(lds + L_ST + (h * NW + wave) * 2) = f32x2{M, l};
    asm volatile("" ::: "memory");
  };

  float m = -INFINITY;
#pragma nounroll
  for (int n = 0; n < 16; ++n) {
    const unsigned on = qoff(n + 1);
    const rsrc_t nr = n < 15 ? kr : vr;
    refresh(n);
    float* prow = lds + L_P + ((n >> 3) * NW + wave) * 512 + (n & 7) * 64 + r * 4;
#pragma unroll
    for (int s = 0; s < 4; ++s) {
      const unsigned early = clk();
      __builtin_amdgcn_sched_barrier(0);
      f32x4 sc;
#pragma unroll
      for (int i = 0; i < 4; ++i) {
        float d = dot2(ring[s].r[i][0], q[0], 0.f);
        d = dot2(ring[s].r[i][1], q[1], d);
        d = dot2(ring[s].r[i][2], q[2], d);
        sc[i] = dot2(ring[s].r[i][3], q[3], d);
      }
      asm volatile("" : "+v"(sc));
      __builtin_amdgcn_sched_barrier(0);
      pace(n * 4 + s + 4, early);
      issue(ring[s], nr, voff, on + s * 16384);
      if (s == 0) mark(n);
#pragma unroll
      for (int i = 0; i < 4; ++i) sc[i] = row16_sum(sc[i]);
      m = fmaxf(fmaxf(m, fmaxf(sc[0], sc[1])), fmaxf(sc[2], sc[3]));
      if (c == 0) *reinterpret_cast<f32x4*>(prow + s * 16) = sc;
      asm volatile("" : "+v"(m) :: "memory");
      __builtin_amdgcn_sched_barrier(0);
    }
    if (n == 7) {
      asm volatile("" :: "v"(cpf));
      convert(0, m);
      m = -INFINITY;
      q = q2;
    }
  }

  f32x2 acc[4] = {{0.f, 0.f}, {0.f, 0.f}, {0.f, 0.f}, {0.f, 0.f}};
#pragma nounroll
  for (int n = 16; n < 32; ++n) {
    const unsigned on = qoff(n + 1);
    const int h = (n >> 3) & 1;
    const float* prow = lds + L_P + (h * NW + wave) * 512 + (n & 7) * 64 + r * 4;
    refresh(n);
    float fs = 0.f;
    if ((n & 7) == 7) {
      const float* st = lds + L_ST + h * NW * 2;
      const f32x4 s01 = *reinterpret_cast<const f32x4*>(st), s23 = *reinterpret_cast<const f32x4*>(st + 4);
      const float MM = fmaxf(fmaxf(s01[0], s01[2]), fmaxf(s23[0], s23[2]));
      const float L = s01[1] * __builtin_amdgcn_exp2f((s01[0] - MM) * C2) + s01[3] * __builtin_amdgcn_exp2f((s01[2] - MM) * C2)
                    + s23[1] * __builtin_amdgcn_exp2f((s23[0] - MM) * C2) + s23[3] * __builtin_amdgcn_exp2f((s23[2] - MM) * C2);
      fs = __builtin_amdgcn_exp2f((st[wave * 2] - MM) * C2) / L;
      asm volatile("" : "+v"(fs));
    }
#pragma unroll
    for (int s = 0; s < 4; ++s) {
      const unsigned early = clk();
      const f32x4 p = *reinterpret_cast<const f32x4*>(prow + s * 16);
      __builtin_amdgcn_sched_barrier(0);
#pragma unroll
      for (int i = 0; i < 4; ++i) {
        const f32x2 p2 = {p[i], p[i]};
#pragma unroll
        for (int j = 0; j < 4; ++j) {
          const unsigned w = ring[s].r[i][j];
          const f32x2 vf = {__builtin_bit_cast(float, w << 16), __builtin_bit_cast(float, w & 0xffff0000u)};
          acc[j] = __builtin_elementwise_fma(p2, vf, acc[j]);
        }
      }
      asm volatile("" : "+v"(acc[0]), "+v"(acc[1]), "+v"(acc[2]), "+v"(acc[3]));
      __builtin_amdgcn_sched_barrier(0);
      pace(n * 4 + s + 4, early);
      issue(ring[s], vr, voff, on + s * 16384);
      if (s == 0) mark(n);
    }
    if (n == CVN) {
      convert(1, m);
      asm volatile("s_waitcnt lgkmcnt(0)\n\ts_barrier" ::: "memory");
    }
    if ((n & 7) == 7) {
      const float b0 = swap32_add(acc[0][0], acc[2][0]), b1 = swap32_add(acc[0][1], acc[2][1]);
      const float b2 = swap32_add(acc[1][0], acc[3][0]), b3 = swap32_add(acc[1][1], acc[3][1]);
      const f32x2 e = {swap16_add(b0, b2) * fs, swap16_add(b1, b3) * fs};
      *reinterpret_cast<f32x2*>(lds + L_O + (h * NW + wave) * D + (4 * c + r) * 2) = e;
      asm volatile("s_waitcnt lgkmcnt(0)\n\ts_barrier" ::: "memory");
      if (wave == 0) {
        const f32x2* po = reinterpret_cast<const f32x2*>(lds + L_O + h * NW * D) + lane;
        const f32x2 o = (po[0] + po[64]) + (po[128] + po[192]);
        const bf16x2 ob = {(__bf16)o[0], (__bf16)o[1]};
        O[(hd + h * NWG) * (D / 2) + lane] = __builtin_bit_cast(unsigned, ob);
      }
#pragma unroll
      for (int j = 0; j < 4; ++j) acc[j] = f32x2{0.f, 0.f};
    }
  }
}

extern "C" void kernel_launch(void* const* d_in, const int* in_sizes, int n_in, void* d_out, int out_size, void* d_ws,
                              size_t ws_size, hipStream_t stream) {
  mha_decode_kernel<<<NWG, NW * 64 + 64, 0, stream>>>((const unsigned char*)d_in[0], (const unsigned char*)d_in[1],
                                                      (const unsigned char*)d_in[2], (unsigned*)d_out, (unsigned*)d_ws);
}
```
